```python
import torch

def _softmax(x, axis=-1):
    return torch.softmax(x, dim=axis)
def _gelu(x):
    return torch.nn.functional.gelu(x, approximate="tanh")
def _silu(x):
    return torch.nn.functional.silu(x)
def _sigmoid(x):
    return torch.sigmoid(x)
def _layernorm(x, axis=-1, eps=1e-5):
    m = x.mean(dim=axis, keepdim=True)
    v = x.var(dim=axis, keepdim=True, unbiased=False)
    return (x - m) / torch.sqrt(v + eps)
def _scatter_add(tgt, idx, src, axis=0):
    return tgt.clone().index_add_(axis, idx, src)
def _segment_sum(src, seg, n):
    out = torch.zeros((n,) + src.shape[1:], dtype=src.dtype)
    return out.index_add_(0, seg, src)
def _pad(x, pw):
    flat = tuple(v for pair in reversed(pw) for v in pair)
    return torch.nn.functional.pad(x, flat)

kernel_name = 'gated_mix__sliding_block__ssm_conv_h3__prompt'

def setup_inputs():

    _w = (lambda a: (lambda t: t.to(torch.bfloat16) if t.is_floating_point() else t)(torch.as_tensor(a)))
    def _A_setup():
        B, S, D = 1, 2048, 576
        return {
            "x": _w((lambda *s: torch.randn(*s, dtype=torch.bfloat16) * 0.05)(B, S, D)),
            "wqkv": _w((lambda *s: torch.randn(*s, dtype=torch.bfloat16) * 0.8)(D, 3 * D)),
            "wo": _w((lambda *s: torch.randn(*s, dtype=torch.bfloat16) * 0.05)(D, D)),
        }
    _A_in = _A_setup()

    def _B_setup():
        B, S, D = 1, 2048, 576
        ks = 4
        return {
            "q": _w((lambda *s: torch.randn(*s, dtype=torch.bfloat16) * 0.05)(B, S, D)),
            "k": _w((lambda *s: torch.randn(*s, dtype=torch.bfloat16) * 0.8)(B, S, D)),
            "v": _w((lambda *s: torch.randn(*s, dtype=torch.bfloat16) * 0.8)(B, S, D)),
            "shift_k": _w((lambda *s: torch.randn(*s, dtype=torch.bfloat16) * 0.8)(D, ks) * 0.5),
            "ssm_k": _w((lambda *s: torch.randn(*s, dtype=torch.bfloat16) * 1.6)(D, S) * (1.0 / S ** 0.5)),
            "ssm_D": _w((lambda *s: torch.randn(*s, dtype=torch.bfloat16) * 0.8)(D)),
        }
    _B_in = _B_setup()

    return {
        "A_x": _A_in["x"],
        "A_wqkv": _A_in["wqkv"],
        "A_wo": _A_in["wo"],
        "B_q": _B_in["q"],
        "B_k": _B_in["k"],
        "B_v": _B_in["v"],
        "B_shift_k": _B_in["shift_k"],
        "B_ssm_k": _B_in["ssm_k"],
        "B_ssm_D": _B_in["ssm_D"],
        "gate_w": _w((lambda *s: torch.randn(*s, dtype=torch.bfloat16) * 0.05)(576) * 16.0),
    }

def reference(A_x, A_wqkv, A_wo, B_q, B_k, B_v, B_shift_k, B_ssm_k, B_ssm_D, gate_w):
    def _A_ref(x, wqkv, wo):
        B, S, D = x.shape
        H = 32; Dh = D // H; W = 256
        qkv = (lambda x, *d: x.permute(*d))((x @ wqkv).reshape(B, S, 3, H, Dh), 2, 0, 3, 1, 4)
        q, k, v = qkv[0], qkv[1], qkv[2]
        scores = torch.einsum("bhqd,bhkd->bhqk", q, k) * (Dh ** -0.5)
        idx = torch.arange(S)
        mask = (idx[:, None] - idx[None, :] >= 0) & (idx[:, None] - idx[None, :] < W)
        scores = torch.where(mask[None, None], scores, -1e9)
        p = _softmax(scores, axis=-1)
        o = torch.einsum("bhqk,bhkd->bhqd", p, v)
        return (lambda x, *d: x.permute(*d))(o, 0, 2, 1, 3).reshape(B, S, D) @ wo

    def _B_ref(q, k, v, shift_k, ssm_k, ssm_D):
        B, S, D = q.shape
        ks = shift_k.shape[-1]
        k_pad = _pad(k, ((0, 0), (ks - 1, 0), (0, 0)))
        k_shift = torch.zeros_like(k)
        for t in range(ks):
            k_shift = k_shift + k_pad[:, t:t + S, :] * shift_k[:, ks - 1 - t][None, None, :]
        kv = k_shift * v
        L = 2 * S
        kv_pad = _pad(kv, ((0, 0), (0, S), (0, 0)))
        kf_pad = _pad(ssm_k, ((0, 0), (0, S)))
        kv_f = (lambda x, axes=(-2,-1): torch.fft.rfft2(x.float(), dim=axes))(kv_pad[:, None, :, :], axes=(1, 2))
        kf_f = (lambda x, axes=(-2,-1): torch.fft.rfft2(x.float(), dim=axes))(kf_pad[:, None, :], axes=(1, 2))
        kf_f = (lambda x, *d: x.permute(*d))(kf_f, 1, 2, 0)
        y_f = kv_f * kf_f[None, :, :, :]
        y = (lambda x, s, axes=(-2,-1): torch.fft.irfft2(x, s=s, dim=axes).to(torch.bfloat16))(y_f, (1, L), axes=(1, 2))[:, 0, :S, :]
        y = y + kv * ssm_D[None, None, :]
        return q * y

    x = A_x
    g = _sigmoid((x * gate_w).sum(-1, keepdims=True))
    a = _A_ref(x, A_wqkv, A_wo)
    b = _B_ref(x, B_k, B_v, B_shift_k, B_ssm_k, B_ssm_D)
    return g * a + (1 - g) * b
```

```cpp
#include <hip/hip_runtime.h>
#include <hip/hip_bf16.h>
#include <stdint.h>

typedef __bf16 bf16x8 __attribute__((ext_vector_type(8)));
typedef __bf16 bf16x2 __attribute__((ext_vector_type(2)));
typedef short s16x4 __attribute__((ext_vector_type(4)));
typedef float f32x4 __attribute__((ext_vector_type(4)));
typedef float f32x2 __attribute__((ext_vector_type(2)));
typedef float f32x16 __attribute__((ext_vector_type(16)));
typedef unsigned u32x4 __attribute__((ext_vector_type(4)));
typedef unsigned u32x2 __attribute__((ext_vector_type(2)));
typedef unsigned short u16;
typedef unsigned long long u64;
#define LAS __attribute__((address_space(3)))
#define GAS __attribute__((address_space(1)))
#define DI __device__ __forceinline__

constexpr int CODE_LINES = 245;
constexpr int S_ = 2048, D_ = 576;
constexpr unsigned WS_Z = 0;
constexpr unsigned WS_UR = WS_Z + 128u * 216 * 256;
constexpr unsigned WS_G = WS_UR + 576u * 4096;
constexpr unsigned WS_OM = WS_G + 2048 * 4;
constexpr unsigned WS_OX = WS_OM + 128u * 64 * 256;
constexpr unsigned WS_YT = WS_OX + 32u * 2048 * 4;
constexpr unsigned WS_WO = WS_YT + 576u * 4096;
constexpr unsigned WS_FL = WS_WO + 8u * 82944;
constexpr int FL_U = 0, FL_G = 96, FL_Z = 208, FL_C = 352, FL_E = 608, FL_A = 609;
constexpr unsigned WS_END = WS_FL + (FL_A + 2048) * 256;

DI unsigned pack2(float a, float b) {
  f32x2 f = {a, b};
  return __builtin_bit_cast(unsigned, __builtin_convertvector(f, bf16x2));
}
DI float blo(unsigned u) { return __builtin_bit_cast(float, u << 16); }
DI float bhi(unsigned u) { return __builtin_bit_cast(float, u & 0xffff0000u); }
DI f32x2 bun(unsigned u) { return f32x2{blo(u), bhi(u)}; }
DI float rbf(float a) { return blo(pack2(a, a)); }
DI f32x4 mfma16(bf16x8 a, bf16x8 b, f32x4 c) { return __builtin_amdgcn_mfma_f32_16x16x32_bf16(a, b, c, 0, 0, 0); }
DI f32x16 mfma32(bf16x8 a, bf16x8 b, f32x16 c) { return __builtin_amdgcn_mfma_f32_32x32x16_bf16(a, b, c, 0, 0, 0); }
DI bf16x8 cat(s16x4 a, s16x4 b) { return __builtin_bit_cast(bf16x8, __builtin_shufflevector(a, b, 0, 1, 2, 3, 4, 5, 6, 7)); }
DI bf16x8 asbf(u32x4 v) { return __builtin_bit_cast(bf16x8, v); }
DI u32x4 lds128(unsigned a) { return *(LAS const u32x4*)(size_t)a; }
DI u32x2 lds64(unsigned a) { return *(LAS const u32x2*)(size_t)a; }
DI f32x4 ldsf4(unsigned a) { return *(LAS const f32x4*)(size_t)a; }
DI s16x4 ldstr(unsigned a) { return __builtin_amdgcn_ds_read_tr16_b64_v4i16((LAS s16x4*)(size_t)a); }
DI void sts128(unsigned a, u32x4 v) { *(LAS u32x4*)(size_t)a = v; }
DI void stsf4(unsigned a, f32x4 v) { *(LAS f32x4*)(size_t)a = v; }
DI void sts64(unsigned a, u32x2 v) { *(LAS u32x2*)(size_t)a = v; }
#define OPAQUE(x) asm volatile("" : "+v"(x))

DI void glds16(unsigned voff, const void* sbase, unsigned lds_dst) {
  unsigned keep;
  asm volatile("s_mov_b32 %0, m0\n\ts_mov_b32 m0, %3\n\ts_nop 0\n\tglobal_load_lds_dwordx4 %1, %2\n\ts_mov_b32 m0, %0"
               : "=&s"(keep) : "v"(voff), "s"(sbase), "s"(lds_dst) : "memory");
}
DI void glds16_sc1(unsigned voff, const void* sbase, unsigned lds_dst) {
  unsigned keep;
  asm volatile("s_mov_b32 %0, m0\n\ts_mov_b32 m0, %3\n\ts_nop 0\n\tglobal_load_lds_dwordx4 %1, %2 sc1\n\ts_mov_b32 m0, %0"
               : "=&s"(keep) : "v"(voff), "s"(sbase), "s"(lds_dst) : "memory");
}
DI void glds16x4(unsigned v0, unsigned v1, unsigned v2, unsigned v3, const void* sbase, unsigned lds_dst) {
  unsigned keep;
  asm volatile("s_mov_b32 %0, m0\n\ts_mov_b32 m0, %6\n\ts_nop 0\n\tglobal_load_lds_dwordx4 %1, %5\n\t"
               "s_add_u32 m0, m0, 0x400\n\ts_nop 0\n\tglobal_load_lds_dwordx4 %2, %5\n\t"
               "s_add_u32 m0, m0, 0x400\n\ts_nop 0\n\tglobal_load_lds_dwordx4 %3, %5\n\t"
               "s_add_u32 m0, m0, 0x400\n\ts_nop 0\n\tglobal_load_lds_dwordx4 %4, %5\n\ts_mov_b32 m0, %0"
               : "=&s"(keep) : "v"(v0), "v"(v1), "v"(v2), "v"(v3), "s"(sbase), "s"(lds_dst) : "memory", "scc");
}
DI void glds16x2(unsigned v0, unsigned v1, const void* sbase, unsigned lds_dst) {
  unsigned keep;
  asm volatile("s_mov_b32 %0, m0\n\ts_mov_b32 m0, %4\n\ts_nop 0\n\tglobal_load_lds_dwordx4 %1, %3\n\t"
               "s_add_u32 m0, m0, 0x400\n\ts_nop 0\n\tglobal_load_lds_dwordx4 %2, %3\n\ts_mov_b32 m0, %0"
               : "=&s"(keep) : "v"(v0), "v"(v1), "s"(sbase), "s"(lds_dst) : "memory", "scc");
}
DI void touch(unsigned voff, const void* sbase, unsigned junk) {
  unsigned keep;
  asm volatile("s_mov_b32 %0, m0\n\ts_mov_b32 m0, %3\n\ts_nop 0\n\tglobal_load_lds_dword %1, %2\n\ts_mov_b32 m0, %0"
               : "=&s"(keep) : "v"(voff), "s"(sbase), "s"(junk) : "memory");
}
#define SB0() __builtin_amdgcn_sched_barrier(0)
#define SGB(m, n) __builtin_amdgcn_sched_group_barrier(m, n, 0)
DI void lgkm_barrier() {
  asm volatile("" ::: "memory");
  __builtin_amdgcn_s_waitcnt(0xC07F);
  asm volatile("s_barrier" ::: "memory");
}
template <int N> DI void wait_vm_barrier() { asm volatile("s_waitcnt vmcnt(%0) lgkmcnt(0)\n\ts_barrier" ::"i"(N) : "memory"); }
DI unsigned lds_arrive(unsigned cnt) {
  unsigned r = 0;
  asm volatile("s_waitcnt vmcnt(0) lgkmcnt(0)" ::: "memory");
  if ((threadIdx.x & 63) == 0) asm volatile("ds_add_rtn_u32 %0, %1, %2\n\ts_waitcnt lgkmcnt(0)" : "=&v"(r) : "v"(cnt), "v"(1u) : "memory");
  return __builtin_amdgcn_readfirstlane(r);
}
template <int N> DI void lds_arrive_n(unsigned cnt) {
  asm volatile("s_waitcnt vmcnt(%0) lgkmcnt(0)" ::"i"(N) : "memory");
  if ((threadIdx.x & 63) == 0) asm volatile("ds_add_u32 %0, %1" ::"v"(cnt), "v"(1u) : "memory");
}
DI void lds_wait(unsigned cnt, unsigned target) {
  for (;;) {
    unsigned f;
    asm volatile("ds_read_b32 %0, %1\n\ts_waitcnt lgkmcnt(0)" : "=&v"(f) : "v"(cnt) : "memory");
    if (__builtin_amdgcn_readfirstlane(f) >= target) break;
    __builtin_amdgcn_s_sleep(2);
  }
}
DI void flag_set(char* ws, int f, u64 tag) {
  if ((threadIdx.x & 63) == 0) __hip_atomic_store((GAS u64*)(ws + WS_FL + f * 256), tag, __ATOMIC_RELAXED, __HIP_MEMORY_SCOPE_AGENT);
}
template <int SLEEP, int SLOW = 0> DI void flag_poll(char* ws, int f, u64 tag) {
  GAS u64* p = (GAS u64*)(ws + WS_FL + f * 256);
#pragma unroll 1
  for (int it = 0; it < (1 << 18); ++it) {
    const u64 v = __hip_atomic_load(p, __ATOMIC_RELAXED, __HIP_MEMORY_SCOPE_AGENT);
    if (__all(v == tag)) break;
    if (SLOW && !__any(v == tag)) __builtin_amdgcn_s_sleep(SLOW);
    else __builtin_amdgcn_s_sleep(SLEEP);
  }
  asm volatile("" ::: "memory");
}
template <int SLEEP, int SLOW = 0> DI void flag_poll_lead(bool lead, unsigned rel, char* ws, int f, u64 tag) {
  if (lead) {
    flag_poll<SLEEP, SLOW>(ws, f, tag);
    asm volatile("ds_write_b32 %0, %1" ::"v"(rel), "v"(1u) : "memory");
  } else lds_wait(rel, 1);
}
typedef __amdgpu_buffer_rsrc_t rsrc_t;
DI void st128_sc1(rsrc_t r, unsigned off, u32x4 v) { __builtin_amdgcn_raw_buffer_store_b128(v, r, off, 0, 16); }
DI void st32_sc1(rsrc_t r, unsigned off, unsigned v) { __builtin_amdgcn_raw_buffer_store_b32(v, r, off, 0, 16); }
DI unsigned ld32_sc1(rsrc_t r, unsigned off) { return __builtin_amdgcn_raw_buffer_load_b32(r, off, 0, 16); }

constexpr unsigned L_K = 0, L_V = 32768;
constexpr unsigned CV_H = 4160, H_SZ = 4 * CV_H, U_SZ = 4608;
constexpr unsigned L_RED = 65536, L_H = 81920, L_U = L_H + 3 * H_SZ, L_RQ = L_U + 3 * U_SZ;
constexpr unsigned L_W = 65536, L_Y = L_W + 82944;
constexpr unsigned L_JUNK = 158720, L_PEEK = L_JUNK + 1024, L_CNT = 163840 - 128;
constexpr unsigned C_Z = L_CNT, C_KV = L_CNT + 4, C_A = L_CNT + 8, C_C0 = L_CNT + 12, C_C1 = L_CNT + 16, C_C2 = L_CNT + 20, C_W = L_CNT + 24,
                   C_B = L_CNT + 28, C_U = L_CNT + 32, C_AE = L_CNT + 36, C_E = L_CNT + 40, C_P2 = L_CNT + 48,
                   C_P3 = L_CNT + 52, C_P4 = L_CNT + 56, C_X = L_CNT + 64;
constexpr unsigned KV_K = 0, KV_V = 9216, KV_SZ = 17408, L_TAPS = 3 * KV_SZ;

DI unsigned lds32(unsigned a) { return *(LAS const unsigned*)(size_t)a; }
struct KvRegs { unsigned kk[7], vv[4]; u32x4 w; };
DI void kv_read(KvRegs& R, int cb, int wave, int lane) {
  const unsigned lp = cb * KV_SZ;
  const int rg = 15 - (lane & 15), cp = lane >> 4;
  const unsigned ka = lp + KV_K + (4 * rg) * 128 + ((wave ^ ((rg + 1) & 7)) * 16) + cp * 4;
  const unsigned kb = lp + KV_K + (4 * rg) * 128 + ((wave ^ ((rg + 2) & 7)) * 16) + cp * 4;
  const unsigned va = lp + KV_V + (4 * rg) * 128 + ((wave ^ (rg & 7)) * 16) + cp * 4;
#pragma unroll
  for (int r = 0; r < 3; ++r) R.kk[r] = lds32(ka + (5 + r) * 128);
#pragma unroll
  for (int r = 3; r < 7; ++r) R.kk[r] = lds32(kb + (5 + r) * 128);
#pragma unroll
  for (int r = 0; r < 4; ++r) R.vv[r] = lds32(va + r * 128);
  R.w = lds128(L_TAPS + (64 * cb + 8 * wave + 2 * cp) * 8);
}
DI void kv_math(const KvRegs& R, rsrc_t wr, int cb, int tb, int wave, int lane) {
  const int cp = lane >> 4;
  const unsigned keep = (tb == 0 && (lane & 15) == 15) ? 0u : ~0u;
  const u32x4 w = R.w;
  const f32x2 w0 = {blo(w[0]), blo(w[2])}, w1 = {bhi(w[0]), bhi(w[2])}, w2 = {blo(w[1]), blo(w[3])}, w3 = {bhi(w[1]), bhi(w[3])};
  f32x2 kf[7], o[4];
#pragma unroll
  for (int r = 0; r < 7; ++r) kf[r] = bun(r < 3 ? R.kk[r] & keep : R.kk[r]);
#pragma unroll
  for (int r = 0; r < 4; ++r) o[r] = (kf[r] * w3 + kf[r + 1] * w2 + kf[r + 2] * w1 + kf[r + 3] * w0) * bun(R.vv[r]);
  const unsigned a0 = pack2(o[3].x, o[2].x), a1 = pack2(o[1].x, o[0].x), b0 = pack2(o[3].y, o[2].y), b1 = pack2(o[1].y, o[0].y);
  const bool odd = lane & 1;
  const unsigned s0 = __builtin_amdgcn_mov_dpp(odd ? a0 : b0, 0xB1, 0xf, 0xf, true), s1 = __builtin_amdgcn_mov_dpp(odd ? a1 : b1, 0xB1, 0xf, 0xf, true);
  const u32x4 v = odd ? u32x4{s0, s1, b0, b1} : u32x4{a0, a1, s0, s1};
  st128_sc1(wr, WS_UR + (64 * cb + 8 * wave + 2 * cp + (lane & 1)) * 4096 + (S_ - 64 * (tb + 1) + 4 * (lane & 14)) * 2, v);
}
DI bf16x8 conv_a(unsigned a) { return cat(ldstr(a), ldstr(a + 8)); }
DI u32x4 conv_pair(f32x4 a, f32x4 b) {
  const auto s0 = __builtin_amdgcn_permlane16_swap(pack2(a[0], a[1]), pack2(b[0], b[1]), false, false);
  const auto s1 = __builtin_amdgcn_permlane16_swap(pack2(a[2], a[3]), pack2(b[2], b[3]), false, false);
  return u32x4{s0[0], s1[0], s0[1], s1[1]};
}
DI unsigned conv_off(int ca, int cb, int lane) { return (256 * ((lane & 16) ? cb : ca) + 16 * (lane & 15) + 8 * (lane >> 5)) * 2; }
DI void conv_stage_h(unsigned p, u32x4 d, u32x2 hp) {
  const unsigned p2 = __builtin_amdgcn_update_dpp(hp[0], d[2], 0x138, 0xf, 0xf, false), p3 = __builtin_amdgcn_update_dpp(hp[1], d[3], 0x138, 0xf, 0xf, false);
  const unsigned a0 = __builtin_amdgcn_alignbit(p3, p2, 16), a1 = __builtin_amdgcn_alignbit(d[0], p3, 16), a2 = __builtin_amdgcn_alignbit(d[1], d[0], 16),
                 a3 = __builtin_amdgcn_alignbit(d[2], d[1], 16), a4 = __builtin_amdgcn_alignbit(d[3], d[2], 16);
  sts128(p, d);
  sts128(p + CV_H, u32x4{a1, a2, a3, a4});
  sts128(p + 2 * CV_H, u32x4{p3, d[0], d[1], d[2]});
  sts128(p + 3 * CV_H, u32x4{a0, a1, a2, a3});
}

__global__ __launch_bounds__(768, 3) void fused(const u16* __restrict__ x, const u16* __restrict__ wqkv, const u16* __restrict__ wo,
                                                const u16* __restrict__ bk, const u16* __restrict__ bv, const u16* __restrict__ shk,
                                                const u16* __restrict__ ssmk, const u16* __restrict__ ssmd, const u16* __restrict__ gw,
                                                char* __restrict__ ws, u16* __restrict__ out) {
  __shared__ __attribute__((aligned(16))) char sm[163840];
  asm volatile("" ::: "memory");
  SB0();
  u64 pc0;
  asm volatile("s_getpc_b64 %0" : "=s"(pc0));
  const int lane = threadIdx.x & 63, pw = __builtin_amdgcn_readfirstlane(threadIdx.x >> 6);
  const int bid = blockIdx.x, xcd = bid & 7, idx = bid >> 3;
  const bool isgemm = idx < 18;
  const unsigned ntab = (xcd & 1) ? 0x47852u : 0x41630u;
  const int mt = 4 * (xcd >> 1) + (idx < 16 ? (idx & 3) : 2 * (xcd & 1) + (idx - 16)), nt = (int)(ntab >> (4 * (idx >> 2))) & 15;
#define K1_SLOT(i) ((((i) + 2) % 3) * 40960)
#define K1_ISSX(i) glds16x4(vx0, vx1, vx2, vx3, xs + (i) * 128, pw * 4096 + K1_SLOT(i))
#define K1_ISSW(i) do { glds16x4(vw[0], vw[1], vw[2], vw[3], wsrc + (i) * 64 * 3456, pw * 6144 + K1_SLOT(i) + 16384); \
    glds16x2(vw[4], vw[5], wsrc + (i) * 64 * 3456, pw * 6144 + K1_SLOT(i) + 16384 + 4096); } while (0)
  const int wave = pw < 4 ? pw + 8 : pw - 4, tid = wave * 64 + lane;
  const int g = lane >> 4, l15 = lane & 15;
  asm volatile("" ::"s"((unsigned)(size_t)(LAS char*)sm));
#define MK_WR() char* wsl_ = ws; asm volatile("" : "+s"(wsl_)); const rsrc_t wr = __builtin_amdgcn_make_buffer_rsrc(wsl_, 0, WS_END, 0x00020000)
  u64 tag;
#define GET_TAG() do { asm volatile("s_load_dwordx2 %0, %1, 0x0\n\ts_waitcnt lgkmcnt(0)" : "=s"(tag) : "s"(ws + WS_FL + FL_E * 256) : "memory"); \
    tag = tag * 6364136223846793005ull + 1442695040888963407ull; } while (0)

  const int cgrp = 8 * xcd + (idx >> 2), qq = idx & 3, ch0 = 9 * cgrp + 2 * qq, chC = 9 * cgrp + 8;
  unsigned dv;
  const int uflag = FL_U + 3 * (lane & 31) + (lane < 32 ? ch0 : chC) / 192;
#define CONV_H_LOAD() do { const unsigned o1_ = (tid - 512) * 16, o2_ = wave == 8 ? 0 : (wave - 8) * 1024 - 8; \
    asm volatile("global_load_dwordx4 %0, %6, %8\n\tglobal_load_dwordx2 %3, %7, %8\n\tglobal_load_dwordx4 %1, %6, %9\n\tglobal_load_dwordx2 %4, %7, %9\n\t" \
                 "global_load_dwordx4 %2, %6, %10\n\tglobal_load_dwordx2 %5, %7, %10" \
                 : "=&v"(hd0), "=&v"(hd1), "=&v"(hd2), "=&v"(hn0), "=&v"(hn1), "=&v"(hn2) \
                 : "v"(o1_), "v"(o2_), "s"(ssmk + ch0 * S_), "s"(ssmk + (ch0 + 1) * S_), "s"(ssmk + chC * S_) : "memory"); \
    asm volatile("global_load_ushort %0, %1, %2" : "=&v"(dv) : "v"((lane < 32 ? ch0 + ((wave - 8) >> 1) : chC) * 2), "s"(ssmd) : "memory"); } while (0)
#define CONV_H_STAGE() do { const int ct_ = tid - 512; unsigned hp_ = L_H + 32 + ct_ * 16; OPAQUE(hp_); \
    if (wave == 8) { hn0 = u32x2{0, 0}; hn1 = hn0; hn2 = hn0; } \
    conv_stage_h(hp_, hd0, hn0); conv_stage_h(hp_ + H_SZ, hd1, hn1); conv_stage_h(hp_ + 2 * H_SZ, hd2, hn2); \
    if (ct_ < 120) sts128(ct_ < 96 ? L_U + (ct_ >> 5) * U_SZ + 4096 + (ct_ & 31) * 16 : L_H + ((ct_ - 96) >> 1) * CV_H + (ct_ & 1) * 16, u32x4{0, 0, 0, 0}); } while (0)
#define CONV_U_LOAD() do { _Pragma("unroll") for (int c_ = 0; c_ < 3; ++c_) \
    glds16_sc1(lane * 16, ws + WS_UR + (unsigned)((c_ < 2 ? ch0 + c_ : chC) * 4096 + (wave - 8) * 1024), L_U + c_ * U_SZ + (wave - 8) * 1024); } while (0)

#define CONV_U_LOAD_ALL() do { _Pragma("unroll") for (int c_ = 0; c_ < 12; ++c_) \
    glds16_sc1(lane * 16, ws + WS_UR + (unsigned)((c_ < 8 ? ch0 + (c_ >> 2) : chC) * 4096 + (c_ & 3) * 1024), L_U + (c_ >> 2) * U_SZ + (c_ & 3) * 1024); } while (0)
  const bool iskv = idx < 30;
  const int kid = 12 * xcd + idx - 18, tb = kid / 3, cb0 = 3 * (kid - 3 * tb);

  if (__builtin_expect(pw < 4, 1)) {
    if (__builtin_expect(isgemm, 1)) {
      unsigned vw[6];
      const unsigned vx0 = (lane >> 3) * 1152 + (((lane & 7) ^ (lane >> 4)) * 16);
      const unsigned vx1 = (vx0 ^ 64) + 8 * 1152, vx2 = vx0 + 16 * 1152, vx3 = vx1 + 16 * 1152;
#pragma unroll
      for (int j = 0; j < 3; ++j) {
        const int q = j * 64 + lane, r = (q * 171) >> 12, c = q - 24 * r;
        vw[j] = r * 3456 + ((c ^ (r & 2)) * 16);
        vw[j + 3] = (vw[j] ^ 64) + 8 * 3456;
      }
      const char* xs = (const char*)x + (unsigned)((128 * mt + 32 * pw) * 1152);
      const char* wsrc = (const char*)wqkv + (unsigned)(16 * pw * 3456 + nt * 384);
      K1_ISSX(0); K1_ISSW(0);
      K1_ISSX(1); K1_ISSW(1);
      GET_TAG();
      u32x4 hd0, hd1, hd2;
      u32x2 hn0, hn1, hn2;
      u64 upeek;
#pragma unroll
      for (int i = 0; i < 7; ++i) {
        wait_vm_barrier<10>();
        K1_ISSX(i + 2); K1_ISSW(i + 2);
        if (i == 6) CONV_H_LOAD();
      }
      asm volatile("s_waitcnt vmcnt(17)\n\ts_barrier\n\tglobal_load_dwordx2 %0, %8, %9 sc1\n\ts_waitcnt vmcnt(8)\n\ts_barrier\n\ts_waitcnt vmcnt(0)"
                   : "=&v"(upeek), "+v"(hd0), "+v"(hd1), "+v"(hd2), "+v"(hn0), "+v"(hn1), "+v"(hn2), "+v"(dv)
                   : "v"((pw == 0 ? uflag : FL_E) * 256), "s"(ws + WS_FL) : "memory");
      __builtin_amdgcn_s_setprio(3);
      const bool uok = __all(upeek == tag);
      if (pw == 0 && uok) CONV_U_LOAD_ALL();
      CONV_H_STAGE();
      if (pw == 0 && !uok) { flag_poll<8>(ws, uflag, tag); CONV_U_LOAD_ALL(); }
    } else {
      const int lw = pw;
#define GEMM_PF(j) do { const unsigned item = min((j) * 56 + lw * 14 + idx - 18, 160), q23 = (item * 179) >> 12, st = 2 + q23, r = item - 23 * q23; \
        const unsigned rw = r - 8, q3 = (rw * 11) >> 5, pn = (ntab >> (4 * (q3 & 7))) & 15; \
        const unsigned ox_ = (512 * (xcd >> 1) + 64 * r) * 1152 + st * 128, ow_ = 64 * st * 3456 + pn * 384 + (rw - 3 * q3) * 128; \
        const u64 m_ = r < 8 ? ~0ull : 0ull; \
        touch(lane * (r < 8 ? 1152 : 3456), (const char*)((((u64)x + ox_) & m_) | (((u64)wqkv + ow_) & ~m_)), L_JUNK); } while (0)
      if (iskv) {
        const int row = lane >> 3, slot = lane & 7;
        const unsigned v0 = row * 1152 + ((slot ^ ((4 * lw + (row >> 2)) & 7)) * 16), v1 = row * 1152 + ((slot ^ ((4 * lw + 2 + (row >> 2)) & 7)) * 16);
        const int kr = 64 * tb - 8 + 16 * lw;
        const char* k0 = (const char*)bk + (unsigned)__builtin_amdgcn_readfirstlane((kr < 0 ? 0 : kr) * 1152 + cb0 * 128);
        const char* k1 = (const char*)bk + (unsigned)((kr + 8) * 1152 + cb0 * 128);
        const char* k8 = (const char*)bk + (unsigned)((64 * tb + 56) * 1152 + cb0 * 128);
        const char* vs = (const char*)bv + (unsigned)((64 * tb + 16 * lw) * 1152 + cb0 * 128);
        if (lw == 0) {
#pragma unroll
          for (int cb = 0; cb < 3; ++cb) glds16(v0, k8 + cb * 128, cb * KV_SZ + KV_K + 8192);
        } else if (lane < 32) glds16(lane * 16, (const char*)shk + (cb0 + lw - 1) * 512, L_TAPS + (lw - 1) * 512);
#pragma unroll
        for (int cb = 0; cb < 3; ++cb) {
          glds16(v0, k0 + cb * 128, cb * KV_SZ + KV_K + lw * 2048); glds16(v1, k1 + cb * 128, cb * KV_SZ + KV_K + lw * 2048 + 1024);
          glds16(v0, vs + cb * 128, cb * KV_SZ + KV_V + lw * 2048); glds16(v1, vs + 8 * 1152 + cb * 128, cb * KV_SZ + KV_V + lw * 2048 + 1024);
        }
      }
      GEMM_PF(0);
      GET_TAG();
      wait_vm_barrier<9>();
      GEMM_PF(1);
      wait_vm_barrier<6>();
      GEMM_PF(2);
      touch(min(((idx - 18) * 4 + lw) * 26 + (lane < 26 ? lane : 0), 1439) * 128, ssmk + 72 * xcd * S_, L_JUNK);
      u32x4 hd0, hd1, hd2;
      u32x2 hn0, hn1, hn2;
      CONV_H_LOAD();
      asm volatile("s_waitcnt vmcnt(11)\n\ts_barrier\n\ts_waitcnt vmcnt(0)" : "+v"(hd0), "+v"(hd1), "+v"(hd2), "+v"(hn0), "+v"(hn1), "+v"(hn2), "+v"(dv) :: "memory");
      CONV_H_STAGE();
      lds_wait(C_U, 8);
      flag_poll_lead<8>(pw == 0, C_X, ws, uflag, tag);
      CONV_U_LOAD();
    }
  } else {
    if (tid < 32) *(LAS unsigned*)(size_t)(L_CNT + tid * 4) = 0;
    MK_WR();
    const int qb = idx & 7, h = 4 * xcd + (idx >> 3);
    u32x4 gxa, gga, gxb, ggb;
#define GATE_LOAD() do { unsigned lo_ = lane * 16; OPAQUE(lo_); const char* xr_ = (const char*)(x + (256 * qb + 32 * wave + h) * D_); \
    gxa = *(const u32x4*)(xr_ + lo_); gga = *(const u32x4*)((const char*)gw + lo_); \
    gxb = *(const u32x4*)(xr_ + 1024 + (lo_ & 112)); ggb = *(const u32x4*)((const char*)gw + 1024 + (lo_ & 112)); } while (0)
#define DPP_ADD(a, ctrl, rm) a += __builtin_bit_cast(float, __builtin_amdgcn_update_dpp(0, __builtin_bit_cast(int, a), ctrl, rm, 0xf, true))
#define GATE_MATH() do { f32x2 a2 = {0.f, 0.f}, b2 = {0.f, 0.f}; \
    _Pragma("unroll") for (int j = 0; j < 4; ++j) { a2 += bun(gxa[j]) * bun(gga[j]); b2 += bun(gxb[j]) * bun(ggb[j]); } \
    float a = a2.x + a2.y + (lane < 8 ? b2.x + b2.y : 0.f); \
    DPP_ADD(a, 0x111, 0xf); DPP_ADD(a, 0x112, 0xf); DPP_ADD(a, 0x114, 0xf); DPP_ADD(a, 0x118, 0xf); DPP_ADD(a, 0x142, 0xa); DPP_ADD(a, 0x143, 0xc); \
    a = rbf(a); \
    const float gv = rbf(__builtin_amdgcn_rcpf(1.0f + __builtin_amdgcn_exp2f(a * -1.4426950408889634f))); \
    st32_sc1(wr, lane == 63 ? WS_G + (256 * qb + 32 * wave + h) * 4 : 0x80000000u, __builtin_bit_cast(unsigned, gv)); } while (0)

    if (__builtin_expect(isgemm, 1)) {
      const int wm = wave & 1, wn = wave >> 1;
      unsigned xa[2][2], wa[2][3];
#pragma unroll
      for (int kk = 0; kk < 2; ++kk) {
        xa[0][kk] = (64 * wm + l15) * 128 + (((4 * kk + g) ^ (l15 >> 1)) * 16);
        xa[1][kk] = xa[0][kk] + 81920;
        OPAQUE(xa[1][kk]);
      }
#pragma unroll
      for (int n = 0; n < 3; ++n) {
        wa[0][n] = 16384 + (8 * g + (l15 >> 2)) * 384 + (((3 * wn + n) ^ (((g & 1) << 1) | (l15 >> 3))) * 32) + (lane & 3) * 8;
        wa[1][n] = wa[0][n] + 81920;
        OPAQUE(wa[0][n]); OPAQUE(wa[1][n]);
      }
      f32x4 acc[4][3];
#pragma unroll
      for (int m = 0; m < 4; ++m)
#pragma unroll
        for (int n = 0; n < 3; ++n) acc[m][n] = f32x4{0, 0, 0, 0};
      unsigned zp = WS_Z + ((24 * nt + 6 * wn + (g >> 1)) * 128 + 8 * mt + 4 * wm + (g & 1)) * 256 + l15 * 16;
      OPAQUE(zp);
      GET_TAG();
      bf16x8 xf[3][4], wf[3][3];
#define G_READ(d, kk) do { _Pragma("unroll") for (int n = 0; n < 3; ++n) { wf[d][n] = cat(ldstr(wa[sh][n] + so + (kk) * 12288), ldstr(wa[sh][n] + so + (kk) * 12288 + 1536)); \
          if (n == 0) { _Pragma("unroll") for (int m = 0; m < 4; ++m) xf[d][m] = asbf(lds128(xa[sh][kk] + so + m * 2048)); } } } while (0)
#define G_MMA(d) do { _Pragma("unroll") for (int n = 0; n < 3; ++n) _Pragma("unroll") for (int m = 0; m < 4; ++m) acc[m][n] = mfma16(wf[d][n], xf[d][m], acc[m][n]); } while (0)
#define G_SCHED() do { _Pragma("unroll") for (int i = 0; i < 12; ++i) { SGB(0x8, 1); SGB(0x100, 1); SGB(0x2, 8); } } while (0)
#pragma unroll
      for (int s = 0; s < 8; ++s) {
        const int slot = (s + 2) % 3, sh = slot >> 1, so = (slot & 1) * 40960;
        SB0();
        lgkm_barrier();
        SB0();
        G_READ(0, 0);
        if (s >= 1) { G_MMA(1); G_SCHED(); }
        SB0();
        G_READ(1, 1);
        G_MMA(0);
        if (s == 2) GATE_MATH();
        G_SCHED();
        if (s == 0) { SB0(); GATE_LOAD(); }
      }
      {
        const int sh = 0, so = 40960;
        SB0();
        lgkm_barrier();
        __builtin_amdgcn_s_setprio(2);
        SB0();
        G_READ(0, 0);
        G_READ(2, 1);
        G_MMA(1);
#pragma unroll
        for (int i = 0; i < 10; ++i) { SGB(0x8, 1); SGB(0x100, 2); }
        SB0();
#pragma unroll
        for (int n = 0; n < 4; ++n) {
          if (n < 3) {
#pragma unroll
            for (int m = 0; m < 4; ++m) acc[m][n] = mfma16(wf[0][n], xf[0][m], acc[m][n]);
#pragma unroll
            for (int m = 0; m < 4; ++m) acc[m][n] = mfma16(wf[2][n], xf[2][m], acc[m][n]);
          }
          if (n >= 1) {
#pragma unroll
            for (int m = 0; m < 4; m += 2) {
              const unsigned a0 = pack2(acc[m][n - 1][0], acc[m][n - 1][1]), a1 = pack2(acc[m][n - 1][2], acc[m][n - 1][3]);
              const unsigned b0 = pack2(acc[m + 1][n - 1][0], acc[m + 1][n - 1][1]), b1 = pack2(acc[m + 1][n - 1][2], acc[m + 1][n - 1][3]);
              const auto s0 = __builtin_amdgcn_permlane16_swap(a0, b0, false, false);
              const auto s1 = __builtin_amdgcn_permlane16_swap(a1, b1, false, false);
              __builtin_amdgcn_raw_buffer_store_b128(u32x4{s0[0], s1[0], s0[1], s1[1]}, wr, zp + m * 256, (n - 1) * 65536, 16);
            }
            if (n < 3) { SGB(0x8, 2); _Pragma("unroll") for (int i = 0; i < 6; ++i) { SGB(0x2, 3); SGB(0x40, 1); SGB(0x8, 1); } }
          }
          SB0();
        }
      }
      if (lds_arrive(C_Z) == 7) flag_set(ws, FL_Z + mt * 9 + nt, tag);
    } else {
      if (wave < 4 && idx >= 30) touch(min(wave * 64 + lane, CODE_LINES) * 128, (const char*)(pc0 & ~127ull), L_JUNK);
      GATE_LOAD();
      GET_TAG();
#pragma unroll 1
      for (int cb = 0; cb < 3; ++cb) {
        lgkm_barrier();
        if (iskv) {
          KvRegs R;
          kv_read(R, cb, wave, lane);
          kv_math(R, wr, cb0 + cb, tb, wave, lane);
        }
      }
      const int q = min((idx - 18) * 384 + min(tid, 383), 5183), ks = q / 288, e = q - 288 * ks;
      const int fg = e < 256 ? (e >> 4) & 3 : (e - 256) >> 3, col = e < 256 ? 16 * (e >> 6) + (e & 15) : 64 + (e & 7);
      const int kp = 32 * ks + 8 * fg;
      if (lds_arrive(C_U) == 7 && iskv) flag_set(ws, FL_U + kid, tag);
      unsigned wsrc = (kp < 512 ? 18 * (kp >> 4) + (kp & 15) : 18 * ((kp - 512) >> 1) + 16) * 1152 + (72 * xcd + col) * 2;
      const unsigned wst = kp < 512 ? 2 * 1152 : 18 * 1152;
      OPAQUE(wsrc);
      u32x4 wv;
#pragma unroll
      for (int j = 0; j < 4; ++j)
        wv[j] = *(const u16*)((const char*)wo + wsrc + j * wst) | ((unsigned)*(const u16*)((const char*)wo + wsrc + j * wst + 1152) << 16);
      st128_sc1(wr, WS_WO + xcd * 82944 + q * 16, wv);
      GATE_MATH();
      if (lds_arrive(C_Z) == 7) flag_set(ws, FL_G + 14 * xcd + idx - 18, tag);
      __builtin_amdgcn_s_sleep(20);
    }
    const int aw = wave;
    __builtin_amdgcn_s_setprio(2);
    const int c0 = (18 * h) >> 3, o2 = ((18 * h) & 7) >> 1;
    const int hh = lane >> 5, l31 = lane & 31;
    const bool halo0 = (qb == 0 && aw < 4);
    f32x16 mlo, mhi, cin;
#pragma unroll
    for (int r = 0; r < 16; ++r) {
      const int key = (r & 3) + 8 * (r >> 2) + 4 * hh;
      mlo[r] = key > l31 ? 0.f : -1e30f;
      mhi[r] = key > l31 ? -1e30f : 0.f;
      cin[r] = 0.f;
    }
    asm volatile("" : "+v"(mlo), "+v"(mhi), "+v"(cin));
    lds_wait(C_Z, 8);
    sts128(L_K + 24576 + aw * 1024 + lane * 16, u32x4{halo0 ? 0xf149u : 0u, 0, 0, 0});
    sts128(L_V + 24576 + aw * 1024 + lane * 16, u32x4{0x3f803f80u, 0x3f803f80u, 0x3f803f80u, 0x3f803f80u});
    if (halo0) {
#pragma unroll
      for (int i = 0; i < 3; ++i) {
        sts128(L_K + i * 8192 + aw * 1024 + lane * 16, u32x4{0, 0, 0, 0});
        sts128(L_V + i * 8192 + aw * 1024 + lane * 16, u32x4{0, 0, 0, 0});
      }
    }
    unsigned kb = L_K + hh * 8192 + 2 * aw * 256 + l31 * 16;
    const int vc = 2 * (g & 1) + ((lane & 3) >> 1);
    unsigned vb0 = L_V + 2 * aw * 256 + vc * 8192 + (((4 * hh + (l15 >> 2)) ^ (4 * vc)) * 16) + (lane & 1) * 8;
    unsigned vb1 = L_V + 2 * aw * 256 + vc * 8192 + (((8 + 4 * hh + (l15 >> 2)) ^ (4 * vc)) * 16) + (lane & 1) * 8;
    unsigned zq = WS_Z + (c0 * 128 + 16 * qb + 2 * aw) * 256 + l31 * 16;
    unsigned omo = WS_OM + ((16 * qb + 2 * aw + (l31 >> 4)) * 64 + 2 * h + hh) * 256 + l15 * 16;
    unsigned oxo = lane < 32 ? WS_OX + ((8 * qb + aw) * 32 + h) * 128 + l31 * 4 : 0x80000000u;
    OPAQUE(kb); OPAQUE(vb0); OPAQUE(vb1); OPAQUE(zq); OPAQUE(omo); OPAQUE(oxo);
    __builtin_amdgcn_s_setprio(0);
    {
      const int sec = (lane >> 1) % 3, ch = 72 * sec + c0 + 2 * (lane & 1);
      const int mq = 2 * qb + (aw >> 2), mk = halo0 ? mq : 2 * qb - 2 + (aw >> 1);
      flag_poll_lead<1, 24>((aw & 1) == 0, C_X + 16 + (aw >> 1) * 4, ws, FL_Z + (sec == 0 ? mq : mk) * 9 + ch / 24, tag);
    }
    __builtin_amdgcn_s_setprio(2);
    u32x4 q1, q2;
    asm volatile("buffer_load_dwordx4 %0, %2, %4, 0 offen sc1\n\tbuffer_load_dwordx4 %1, %3, %4, 0 offen sc1"
                 : "=&v"(q1), "=&v"(q2) : "v"(zq + hh * 32768), "v"(zq + 65536), "s"(wr) : "memory");
    {
      const char* zb = ws + WS_Z + (unsigned)((c0 * 128 + 16 * qb - (halo0 ? 0 : 16) + 4 * aw) * 256);
#pragma unroll
      for (int i = 0; i < 3; ++i) glds16_sc1(lane * 16, zb + (unsigned)((72 + i) * 32768), halo0 ? L_JUNK : L_K + i * 8192 + aw * 1024);
#pragma unroll
      for (int i = 0; i < 3; ++i) glds16_sc1((lane ^ (4 * i)) * 16, zb + (unsigned)((144 + i) * 32768), halo0 ? L_JUNK : L_V + i * 8192 + aw * 1024);
    }
    asm volatile("s_waitcnt vmcnt(6)" : "+v"(q1), "+v"(q2) :: "memory");
#pragma unroll
    for (int d = 0; d < 4; ++d) {
      const float QS = 0.23570226039551584f * 1.4426950408889634f;
      q1[d] = (hh == 0 && d < o2) ? 0u : pack2(blo(q1[d]) * QS, bhi(q1[d]) * QS);
      q2[d] = hh ? (d == 0 ? 0x3f80u : 0u) : (d <= o2 ? pack2(blo(q2[d]) * QS, bhi(q2[d]) * QS) : 0u);
    }
    asm volatile("" : "+v"(q1), "+v"(q2));
    lds_arrive_n<0>(C_KV);
    __builtin_amdgcn_s_setprio(0);
    lds_wait(C_KV, 8);
    f32x16 o;
    bool use_max = false;
    for (;;) {
      if (use_max) {
        float m = -3e38f;
#pragma unroll
        for (int kt = 0; kt < 9; ++kt) {
          f32x16 s = mfma32(asbf(lds128(kb + kt * 512)), asbf(q1), kt == 0 ? mlo : (kt == 8 ? mhi : cin));
          s = mfma32(asbf(lds128(kb + kt * 512 + 16384)), asbf(q2), s);
#pragma unroll
          for (int r = 0; r < 16; r += 2) m = fmaxf(fmaxf(m, s[r]), s[r + 1]);
        }
        const unsigned mu = __builtin_bit_cast(unsigned, m);
        const auto r32 = __builtin_amdgcn_permlane32_swap(mu, mu, false, false);
        const unsigned ma = r32[0], mb = r32[1];
        m = fmaxf(__builtin_bit_cast(float, ma), __builtin_bit_cast(float, mb));
#pragma unroll
        for (int r = 0; r < 16; ++r) { cin[r] = -m; mlo[r] -= m; mhi[r] -= m; }
        asm volatile("" ::: "memory");
      }
#pragma unroll
      for (int r = 0; r < 16; ++r) o[r] = 0.f;
      u32x4 ka = lds128(kb), kc = lds128(kb + 16384);
      f32x16 sc = mfma32(asbf(kc), asbf(q2), mfma32(asbf(ka), asbf(q1), mlo));
      ka = lds128(kb + 512); kc = lds128(kb + 512 + 16384);
      bf16x8 vf0 = cat(ldstr(vb0), ldstr(vb1)), vf1 = cat(ldstr(vb0 + 256), ldstr(vb1 + 256));
      u32x4 pl;
      bf16x8 vl;
#pragma unroll
      for (int kt = 0; kt < 9; ++kt) {
        SB0();
        f32x16 sn;
        u32x4 kan, kcn;
        bf16x8 vn0, vn1;
        if (kt > 0) o = mfma32(vl, asbf(pl), o);
        if (kt < 8) {
          sn = mfma32(asbf(kc), asbf(q2), mfma32(asbf(ka), asbf(q1), kt == 7 ? mhi : cin));
          vn0 = cat(ldstr(vb0 + (2 * kt + 2) * 256), ldstr(vb1 + (2 * kt + 2) * 256));
          vn1 = cat(ldstr(vb0 + (2 * kt + 3) * 256), ldstr(vb1 + (2 * kt + 3) * 256));
        }
        if (kt < 7) { kan = lds128(kb + (kt + 2) * 512); kcn = lds128(kb + (kt + 2) * 512 + 16384); }
        unsigned P[8];
#pragma unroll
        for (int r = 0; r < 8; ++r) P[r] = pack2(__builtin_amdgcn_exp2f(sc[2 * r]), __builtin_amdgcn_exp2f(sc[2 * r + 1]));
        o = mfma32(vf0, asbf(u32x4{P[0], P[1], P[2], P[3]}), o);
        pl = u32x4{P[4], P[5], P[6], P[7]}; vl = vf1;
        if (kt > 0) SGB(0x8, 1);
        SGB(0x100, 2); SGB(0x400, 4);
        if (kt < 8) SGB(0x8, 1);
        SGB(0x100, 2); SGB(0x400, 4); SGB(0x2, 4);
        if (kt < 8) SGB(0x8, 1);
        SGB(0x100, 2); SGB(0x400, 4); SGB(0x8, 1); SGB(0x400, 4); SGB(0x2, 4);
        if (kt < 8) { sc = sn; vf0 = vn0; vf1 = vn1; ka = kan; kc = kcn; }
      }
      SB0();
      o = mfma32(vl, asbf(pl), o);
      if (use_max) break;
      if (__all(o[12] > 1e-18f && o[12] < 1e18f)) break;
      use_max = true;
    }
    __builtin_amdgcn_s_setprio(3);
    {
      const float inv = __builtin_amdgcn_rcpf(o[12]);
      unsigned Dw[12];
#pragma unroll
      for (int i = 0; i < 6; ++i) {
        const unsigned pk = pack2(o[2 * i] * inv, o[2 * i + 1] * inv);
        const auto sw = __builtin_amdgcn_permlane32_swap(pk, pk, false, false);
        Dw[4 * (i >> 1) + (i & 1)] = sw[0];
        Dw[4 * (i >> 1) + 2 + (i & 1)] = sw[1];
      }
      u32x4 mv;
      unsigned ex;
#define O_PICK(K) do { mv = hh ? u32x4{Dw[K + 4], Dw[K + 5], Dw[K + 6], Dw[K + 7]} : u32x4{Dw[K], Dw[K + 1], Dw[K + 2], Dw[K + 3]}; ex = Dw[K + 8]; } while (0)
      if (o2 == 0) O_PICK(0); else if (o2 == 1) O_PICK(1); else if (o2 == 2) O_PICK(2); else O_PICK(3);
      st128_sc1(wr, omo, mv);
      st32_sc1(wr, oxo, ex);
    }
    lds_arrive_n<63>(aw < 4 ? C_AE : C_A);
    asm volatile("s_waitcnt vmcnt(0)" ::: "memory");
    flag_set(ws, FL_A + (qb * 8 + aw) * 32 + h, tag);
    if (bid == 0 && wave < 4) {
      flag_poll<8>(ws, FL_G + wave * 64 + lane, tag);
      if (lds_arrive(C_E) == 3) flag_set(ws, FL_E, tag);
    }
    return;
  }

  const int cw = wave - 8;
  MK_WR();
  {
    const int chm = ch0 + (cw >> 1);
    const unsigned aoff = L_H + (3 - (l15 >> 2)) * CV_H + 2 * (4 * (l15 & 3) + 8 * g + 4);
    const unsigned boff = L_U + 4064 - 3584 - 32 * l15 + 16 * g;
    unsigned ab = (cw >> 1) * H_SZ + (cw & 1) * 256 + aoff, bb = (cw >> 1) * U_SZ + (cw & 1) * 256 + boff;
    OPAQUE(ab); OPAQUE(bb);
    u32x4 dgm, dgc;
    {
      const unsigned dm = (cw & 1) ? 0u : __builtin_amdgcn_readlane(dv, 0), dc = cw ? 0u : __builtin_amdgcn_readlane(dv, 63);
      const int pos = 15 - l15 - 8 * g;
#pragma unroll
      for (int i = 0; i < 4; ++i) {
        dgm[i] = (pos >> 1) == i ? dm << (16 * (pos & 1)) : 0u;
        dgc[i] = (pos >> 1) == i ? dc << (16 * (pos & 1)) : 0u;
      }
    }
    unsigned qa[9], qbb[9], qd;
    {
      const unsigned ac = 2 * H_SZ + cw * 128 + aoff, bc = 2 * U_SZ + cw * 128 + boff;
      unsigned bq = bc + 512 * qq, a0 = ac;
      OPAQUE(bq); OPAQUE(a0);
#pragma unroll
      for (int j = 0; j < 5; ++j) { qa[j] = a0 + 512 * j; qbb[j] = bq + 512 * j; }
#pragma unroll
      for (int j = 5; j < 9; ++j) {
        const bool first = j <= 7 - qq;
        qa[j] = ac + 512 * (first ? j : j - 8 + qq);
        qbb[j] = bc + 512 * (first ? qq + j : j - 1);
        OPAQUE(qa[j]); OPAQUE(qbb[j]);
      }
      qd = bc + 512 * (7 - qq);
    }
    unsigned red = L_RED + lane * 16 + cw * 4096, rq = L_RQ + lane * 16;
    OPAQUE(red); OPAQUE(rq);
    asm volatile("" : "+v"(dgm), "+v"(dgc));
    lds_arrive_n<0>(C_C0);
    if (lane < 14) glds16_sc1((FL_G + 14 * (idx >> 2) + lane) * 256, ws + WS_FL, L_PEEK + cw * 256);
    __builtin_amdgcn_s_setprio(1);
    lds_wait(C_C0, 4);
    f32x4 acc[8];
    {
      bf16x8 A[4][8], B[4][8], QA[2][9], QB[2][9];
      const f32x4 z4 = {0, 0, 0, 0};
#define CV_LD(rr, i) do { A[rr][i] = conv_a(ab + 64 * (rr) + 512 * (7 - (i))); B[rr][i] = asbf(lds128(bb + 64 * (rr) + 512 * (7 - (i)))); } while (0)
#define CV_LQ(rr, j) do { QA[rr][j] = conv_a(qa[j] + 64 * (rr)); QB[rr][j] = asbf(lds128(qbb[j] + 64 * (rr))); } while (0)
      const bf16x8 bd = asbf(lds128(qd));
      CV_LQ(0, 0); CV_LQ(0, 1); CV_LQ(0, 2); CV_LQ(0, 3);
      f32x4 aq[5] = {mfma16(asbf(dgc), QB[0][0], z4), z4, z4, z4, mfma16(asbf(dgc), bd, z4)};
#pragma unroll
      for (int n = 0; n < 18; ++n) {
        SB0();
        if (n + 4 < 18) CV_LQ((n + 4) / 9, (n + 4) % 9); else CV_LD(0, n - 14);
        if (n == 17) CV_LD(0, 4);
        const int j = n % 9, ai = j < 5 ? 0 : j - 4;
        aq[ai] = mfma16(QA[n / 9][j], QB[n / 9][j], aq[ai]);
        SGB(0x8, 1); SGB(0x100, 6);
      }
#pragma unroll
      for (int c = 0; c < 8; ++c) acc[c] = z4;
#pragma unroll
      for (int rr = 0; rr < 4; ++rr) {
#pragma unroll
        for (int i = 0; i < 8; ++i) {
          SB0();
          if (i >= 2 && i <= 4) CV_LD(rr, i + 3);
          if (rr < 3) {
            if (i == 5) CV_LD(rr + 1, 0);
            if (i == 6) { CV_LD(rr + 1, 1); CV_LD(rr + 1, 2); }
            if (i == 7) { CV_LD(rr + 1, 3); CV_LD(rr + 1, 4); }
          }
#pragma unroll
          for (int j = 0; j <= i; ++j) acc[7 - i + j] = mfma16(A[rr][i], B[rr][j], acc[7 - i + j]);
          if (rr == 0 && i == 7) {
#pragma unroll
            for (int j = 0; j < 8; ++j) acc[j] = mfma16(asbf(dgm), B[0][j], acc[j]);
            f32x4 a1 = aq[0], a2 = aq[4];
#pragma unroll
            for (int j = 5; j < 8; ++j) { if (j <= 7 - qq) a1 += aq[j - 4]; else a2 += aq[j - 4]; }
            stsf4(rq + cw * 1024, a1);
            stsf4(rq + 4096 + cw * 1024, a2);
#pragma unroll
            for (int j = 0; j < 16; ++j) { SGB(0x8, 1); SGB(0x100, 1); SGB(0x2, 3); }
          } else {
#pragma unroll
            for (int j = 0; j <= i; ++j) { SGB(0x8, 1); SGB(0x100, 1); }
          }
          if (rr == 2 && i == 7) { SB0(); if (isgemm) lds_wait(C_Z, 8); }
        }
      }
      SB0();
    }
    __builtin_amdgcn_s_setprio(3);
    u32x4 yo[3];
#define CV_FIN(K, S) do { _Pragma("unroll") for (int i = 0; i < 4; ++i) stsf4(red + i * 1024, acc[S + i]); \
      lds_arrive_n<63>(C_C1); lds_wait(C_C1, 4); \
      f32x4 pr[4]; _Pragma("unroll") for (int i = 0; i < 4; ++i) pr[i] = ldsf4((red ^ 4096) + i * 1024); \
      SB0(); \
      yo[0] = conv_pair(acc[K] + pr[0], acc[K + 1] + pr[1]); yo[1] = conv_pair(acc[K + 2] + pr[2], acc[K + 3] + pr[3]); } while (0)
    if (cw & 1) CV_FIN(4, 0); else CV_FIN(0, 4);
    if (cw == 3) {
      f32x4 pr[8];
#pragma unroll
      for (int i = 0; i < 8; ++i) pr[i] = ldsf4(rq + i * 1024);
      SB0();
      yo[2] = conv_pair((pr[0] + pr[1]) + (pr[2] + pr[3]), (pr[4] + pr[5]) + (pr[6] + pr[7]));
    }
    asm volatile("s_waitcnt vmcnt(0)" ::: "memory");
    const bool gok = __all(__builtin_bit_cast(u64, lds64(L_PEEK + cw * 256 + min(lane & 15, 13) * 16)) == tag);
    asm volatile("" : "+v"(yo[0]), "+v"(yo[1]), "+v"(yo[2]));
    lds_arrive_n<63>(C_B);
    __builtin_amdgcn_s_setprio(1);
    if (!gok) flag_poll<16>(ws, FL_G + 14 * (idx >> 2) + min(lane & 15, 13), tag);
#define YT_STORE() do { st128_sc1(wr, WS_YT + chm * 4096 + conv_off((cw & 1) * 4, (cw & 1) * 4 + 1, lane), yo[0]); \
      st128_sc1(wr, WS_YT + chm * 4096 + conv_off((cw & 1) * 4 + 2, (cw & 1) * 4 + 3, lane), yo[1]); \
      if (cw == 3) st128_sc1(wr, WS_YT + chC * 4096 + conv_off(7 - qq, qq, lane), yo[2]); } while (0)
    YT_STORE();
    lds_wait(C_KV, 8);
    lds_wait(C_B, 4);
    {
      const char* wsl = ws + WS_WO + (unsigned)((idx >> 2) * 82944);
#pragma unroll
      for (int i = 0; i < 20; ++i) {
        int j = cw + 4 * i + 20 * (idx & 3);
        j = j >= 80 ? j - 80 : j;
        glds16_sc1(lane * 16, wsl + (unsigned)(j * 1024), L_W + j * 1024);
        if (i == 1) {
          unsigned r = 0;
          asm volatile("s_waitcnt vmcnt(2)" ::: "memory");
          if (lane == 0) asm volatile("ds_add_rtn_u32 %0, %1, %2\n\ts_waitcnt lgkmcnt(0)" : "=&v"(r) : "v"(C_C2), "v"(1u) : "memory");
          if (__builtin_amdgcn_readfirstlane(r) == 3) flag_set(ws, FL_C + 32 * xcd + idx, tag);
        }
      }
      if (cw == 0) glds16_sc1(lane * 16, wsl + 80 * 1024, L_W + 80 * 1024);
    }
  }

  {
    const int oq = xcd, oj = idx & 3, cbk = idx >> 2, late = cw >> 1;
    const int tile = 16 * oq + 2 * oj + (cw & 1) + 8 * late, trow = 16 * tile + l15;
    const unsigned yb = L_Y + cw * 2560;
    unsigned oo[3];
#pragma unroll
    for (int i = 0; i < 3; ++i) {
      const int q = i * 64 + lane, r = q / 9;
      oo[i] = (16 * tile + r) * 1152 + cbk * 144 + (q - 9 * r) * 16;
      OPAQUE(oo[i]);
    }
    glds16(oo[0], x, yb);
    glds16(oo[1], x, yb + 1024);
    if (lane < 16) glds16(oo[2], x, yb + 2048);
    lds_arrive_n<0>(C_W);
    u32x2 xv[5], bt[5];
#pragma unroll
    for (int n = 0; n < 5; ++n) xv[n] = lds64(yb + l15 * 144 + n * 32 + g * 8);
    asm volatile("s_waitcnt lgkmcnt(0)" : "+v"(xv[0]), "+v"(xv[1]), "+v"(xv[2]), "+v"(xv[3]), "+v"(xv[4]) :: "memory");
#define Y_LOAD() do { _Pragma("unroll") for (int i = 0; i < 3; ++i) { const int row = 32 * i + (lane >> 1); \
      if (row < 72) glds16_sc1(row * 4096 + (lane & 1) * 16, ws + WS_YT + (unsigned)(72 * cbk * 4096 + tile * 32), yb + i * 1024); } } while (0)
    {
      flag_poll_lead<6>(cw == 0, C_P2, ws, FL_C + 32 * cbk + (lane & 31), tag);
      Y_LOAD();
    }
    unsigned wa[2], wb[2];
#pragma unroll
    for (int i = 0; i < 2; ++i) {
      wa[i] = L_W + lane * 16 + i * 9 * 4608; wb[i] = L_W + 4096 + (g * 8 + (lane & 7)) * 16 + i * 9 * 4608;
      OPAQUE(wa[i]); OPAQUE(wb[i]);
    }
    unsigned om = WS_OM + tile * 16384 + lane * 16, ox = WS_OX + ((tile >> 1) * 32 + 4 * g) * 128 + (trow & 31) * 4;
    OPAQUE(om); OPAQUE(ox);
    lds_wait(C_W, 4);
    bf16x8 wf[2][5];
#define W_LD(b, s) do { _Pragma("unroll") for (int n = 0; n < 4; ++n) wf[b][n] = asbf(lds128(wa[(s) / 9] + ((s) % 9) * 4608 + n * 1024)); \
      wf[b][4] = asbf(lds128(wb[(s) / 9] + ((s) % 9) * 4608)); } while (0)
    W_LD(0, 0);
    asm volatile("s_waitcnt vmcnt(0)" ::: "memory");
#pragma unroll
    for (int n = 0; n < 5; ++n) {
      const u32x2 y = __builtin_bit_cast(u32x2, ldstr(yb + lane * 8 + n * 512));
      bt[n] = u32x2{pack2(blo(xv[n][0]) * blo(y[0]), bhi(xv[n][0]) * bhi(y[0])), pack2(blo(xv[n][1]) * blo(y[1]), bhi(xv[n][1]) * bhi(y[1]))};
    }
    asm volatile("" : "+v"(bt[0]), "+v"(bt[1]), "+v"(bt[2]), "+v"(bt[3]), "+v"(bt[4]));
    lds_wait(late ? C_A : C_AE, 4);
    flag_poll_lead<1>((cw & 1) == 0, late ? C_P3 : C_P4, ws, FL_A + (oq * 8 + oj + 4 * late) * 32 + (lane & 31), tag);
    u32x4 of[18];
#pragma unroll
    for (int s = 0; s < 16; ++s) of[s] = __builtin_amdgcn_raw_buffer_load_b128(wr, om + (s & 3) * 1024, (s >> 2) * 4096, 16);
#pragma unroll
    for (int i = 0; i < 8; ++i) of[16 + (i >> 2)][i & 3] = ld32_sc1(wr, ox + (16 * (i >> 2) + (i & 3)) * 128);
    const float gt = __builtin_bit_cast(float, ld32_sc1(wr, WS_G + trow * 4));
    f32x4 acc[5];
#pragma unroll
    for (int n = 0; n < 5; ++n) acc[n] = f32x4{0, 0, 0, 0};
#pragma unroll
    for (int s = 0; s < 18; ++s) {
      SB0();
      if (s < 17) W_LD((s + 1) & 1, s + 1);
#pragma unroll
      for (int n = 0; n < 5; ++n) acc[n] = mfma16(wf[s & 1][n], asbf(of[s]), acc[n]);
      SGB(0x100, 2); SGB(0x8, 1); SGB(0x100, 1); SGB(0x8, 1); SGB(0x100, 1); SGB(0x8, 1); SGB(0x100, 1); SGB(0x8, 2);
    }
    SB0();
#pragma unroll
    for (int n = 0; n < 5; ++n) {
      const f32x4 bb = {blo(bt[n][0]), bhi(bt[n][0]), blo(bt[n][1]), bhi(bt[n][1])};
      const f32x4 r = (acc[n] - bb) * gt + bb;
      if (n < 4 || g < 2) sts64(yb + l15 * 144 + n * 32 + g * 8, u32x2{pack2(r[0], r[1]), pack2(r[2], r[3])});
    }
    __builtin_amdgcn_wave_barrier();
#pragma unroll
    for (int i = 0; i < 3; ++i)
      if (i < 2 || lane < 16)
        __builtin_amdgcn_raw_buffer_store_b128(lds128(yb + (i * 64 + lane) * 16), __builtin_amdgcn_make_buffer_rsrc(out, 0, S_ * D_ * 2, 0x00020000), oo[i], 0, 16);
  }
}

extern "C" void kernel_launch(void* const* d_in, const int* in_sizes, int n_in, void* d_out, int out_size, void* d_ws,
                              size_t ws_size, hipStream_t stream) {
  fused<<<256, 768, 0, stream>>>((const u16*)d_in[0], (const u16*)d_in[1], (const u16*)d_in[2], (const u16*)d_in[4],
                                 (const u16*)d_in[5], (const u16*)d_in[6], (const u16*)d_in[7], (const u16*)d_in[8],
                                 (const u16*)d_in[9], (char*)d_ws, (u16*)d_out);
}
```
